```python
import torch

def _softmax(x, axis=-1):
    return torch.softmax(x, dim=axis)
def _gelu(x):
    return torch.nn.functional.gelu(x, approximate="tanh")
def _silu(x):
    return torch.nn.functional.silu(x)
def _sigmoid(x):
    return torch.sigmoid(x)
def _layernorm(x, axis=-1, eps=1e-5):
    m = x.mean(dim=axis, keepdim=True)
    v = x.var(dim=axis, keepdim=True, unbiased=False)
    return (x - m) / torch.sqrt(v + eps)
def _scatter_add(tgt, idx, src, axis=0):
    return tgt.clone().index_add_(axis, idx, src)
def _segment_sum(src, seg, n):
    out = torch.zeros((n,) + src.shape[1:], dtype=src.dtype)
    return out.index_add_(0, seg, src)
def _pad(x, pw):
    flat = tuple(v for pair in reversed(pw) for v in pair)
    return torch.nn.functional.pad(x, flat)

kernel_name = 'recurrent_scan__mha_block_cached__swiglu_ffn__decode'

def setup_inputs():

    _w = (lambda a: (lambda t: t.to(torch.bfloat16) if t.is_floating_point() else t)(torch.as_tensor(a)))
    def _A_setup():
        B, Sq, D = 4, 1, 512
        H, Sk, Dh = 32, 8192, 128
        return {
            "x": _w((lambda *s: torch.randn(*s, dtype=torch.bfloat16) * 0.05)(B, Sq, D)),
            "k_cache": _w((lambda *s: torch.randn(*s, dtype=torch.bfloat16) * 1.6)(B, H, Sk, Dh)),
            "v_cache": _w((lambda *s: torch.randn(*s, dtype=torch.bfloat16) * 0.2)(B, H, Sk, Dh)),
            "wq": _w((lambda *s: torch.randn(*s, dtype=torch.bfloat16) * 1.6)(D, H * Dh) * (D ** -0.5)),
            "wo": _w((lambda *s: torch.randn(*s, dtype=torch.bfloat16) * 0.2)(H * Dh, D) * ((H * Dh) ** -0.5)),
        }
    _A_in = _A_setup()

    def _B_setup():
        B, S, D, F = 4, 1, 512, 1792
        return {
            "x": _w((lambda *s: torch.randn(*s, dtype=torch.bfloat16) * 0.05)(B, S, D)),
            "w_gate": _w((lambda *s: torch.randn(*s, dtype=torch.bfloat16) * 0.1)(D, F)),
            "w_up": _w((lambda *s: torch.randn(*s, dtype=torch.bfloat16) * 0.1)(D, F)),
            "w_down": _w((lambda *s: torch.randn(*s, dtype=torch.bfloat16) * 0.1)(F, D)),
        }
    _B_in = _B_setup()

    return {
        "A_x": _A_in["x"],
        "A_k_cache": _A_in["k_cache"],
        "A_v_cache": _A_in["v_cache"],
        "A_wq": _A_in["wq"],
        "A_wo": _A_in["wo"],
        "B_x": _B_in["x"],
        "B_w_gate": _B_in["w_gate"],
        "B_w_up": _B_in["w_up"],
        "B_w_down": _B_in["w_down"],
    }

def reference(A_x, A_k_cache, A_v_cache, A_wq, A_wo, B_x, B_w_gate, B_w_up, B_w_down):
    def _A_ref(x, k_cache, v_cache, wq, wo):
        B, Sq, D = x.shape
        H, Dh = 32, 128
        q = (lambda x, *d: x.permute(*d))((x @ wq).reshape(B, Sq, H, Dh), 0, 2, 1, 3)
        scores = torch.einsum("bhqd,bhkd->bhqk", q, k_cache) * (Dh ** -0.5)
        p = _softmax(scores, axis=-1)
        o = torch.einsum("bhqk,bhkd->bhqd", p, v_cache)
        return (lambda x, *d: x.permute(*d))(o, 0, 2, 1, 3).reshape(B, Sq, H * Dh) @ wo

    def _B_ref(x, w_gate, w_up, w_down):
        g = _silu(x @ w_gate)
        u = x @ w_up
        return (g * u) @ w_down

    x = A_x
    xn = _layernorm(x, axis=-1)
    s = _A_ref(xn, A_k_cache, A_v_cache, A_wq, A_wo)
    g = _silu(_B_ref(xn, B_w_gate, B_w_up, B_w_down))
    return x + s * g
```

```cpp
#include <hip/hip_runtime.h>
#include <hip/hip_bf16.h>
#include <stdint.h>

typedef unsigned short u16;
typedef unsigned v4u __attribute__((ext_vector_type(4)));
typedef __bf16 bf16x2 __attribute__((ext_vector_type(2)));
constexpr int B = 4, D = 512, H = 32, SK = 8192, DH = 128, F = 1792;
constexpr int NSPLIT = 4;

__device__ __forceinline__ float bf2f(u16 v) { return __uint_as_float((unsigned)v << 16); }
__device__ __forceinline__ u16 f2bf(float f) { __hip_bfloat16 b = __float2bfloat16(f); return *(u16*)&b; }
__device__ __forceinline__ float rbf(float f) { return bf2f(f2bf(f)); }

constexpr int WS_XN = 0, WS_Q = WS_XN + B * D, WS_H = WS_Q + B * H * DH, WS_G = WS_H + B * F, WS_P = WS_G + B * D, WS_O = WS_P + B * H * NSPLIT * 132;

__global__ void k_ln(const u16* x, float* ws) {
  __shared__ float red[8];
  int b = blockIdx.x, t = threadIdx.x;
  float v = bf2f(x[b * D + t]);
  float s = v;
  for (int o = 32; o; o >>= 1) s += __shfl_xor(s, o);
  if ((t & 63) == 0) red[t >> 6] = s;
  __syncthreads();
  float tot = 0; for (int i = 0; i < 8; ++i) tot += red[i];
  float mean = tot / D;
  __syncthreads();
  float d = v - mean; s = d * d;
  for (int o = 32; o; o >>= 1) s += __shfl_xor(s, o);
  if ((t & 63) == 0) red[t >> 6] = s;
  __syncthreads();
  tot = 0; for (int i = 0; i < 8; ++i) tot += red[i];
  float var = rbf(tot / D);
  float m = rbf(mean);
  float num = rbf(v - m);
  float den = rbf(sqrtf(rbf(var + 1e-5f)));
  ws[WS_XN + b * D + t] = rbf(num / den);
}
__global__ void k_proj(const u16* wq, const u16* wg, const u16* wu, float* ws) {
  __shared__ float xn[B * D];
  for (int i = threadIdx.x; i < B * D; i += blockDim.x) xn[i] = ws[WS_XN + i];
  __syncthreads();
  int c = blockIdx.x * blockDim.x + threadIdx.x;
  if (c < H * DH) {
    float a[B] = {0, 0, 0, 0};
    for (int k = 0; k < D; ++k) { float w = bf2f(wq[k * H * DH + c]); for (int b = 0; b < B; ++b) a[b] += xn[b * D + k] * w; }
    for (int b = 0; b < B; ++b) ws[WS_Q + b * H * DH + c] = rbf(a[b]);
  } else if (c < H * DH + F) {
    c -= H * DH;
    float a[B] = {0, 0, 0, 0}, u[B] = {0, 0, 0, 0};
    for (int k = 0; k < D; ++k) { float w = bf2f(wg[k * F + c]), w2 = bf2f(wu[k * F + c]); for (int b = 0; b < B; ++b) { a[b] += xn[b * D + k] * w; u[b] += xn[b * D + k] * w2; } }
    for (int b = 0; b < B; ++b) { float g = rbf(a[b]); g = rbf(g / (1.f + __expf(-g))); ws[WS_H + b * F + c] = rbf(g * rbf(u[b])); }
  }
}
__global__ void k_down(const u16* wd, float* ws) {
  int c = blockIdx.x * blockDim.x + threadIdx.x;
  float a[B] = {0, 0, 0, 0};
  for (int k = 0; k < F; ++k) { float w = bf2f(wd[k * D + c]); for (int b = 0; b < B; ++b) a[b] += ws[WS_H + b * F + k] * w; }
  for (int b = 0; b < B; ++b) { float g = rbf(a[b]); ws[WS_G + b * D + c] = rbf(g / (1.f + __expf(-g))); }
}
__device__ __forceinline__ float dot2(unsigned k, unsigned q, float acc) {
  return __builtin_amdgcn_fdot2_f32_bf16(__builtin_bit_cast(bf16x2, k), __builtin_bit_cast(bf16x2, q), acc, false);
}
__global__ void __launch_bounds__(256) k_attn(const u16* K, const u16* V, float* ws) {
  __shared__ float sm[16][132];
  int bh = blockIdx.x / NSPLIT, s = blockIdx.x % NSPLIT;
  int t = threadIdx.x, lane = t & 63, w = t >> 6, r = lane >> 4, c = lane & 15;
  unsigned qp[4];
  for (int i = 0; i < 4; ++i) qp[i] = (unsigned)f2bf(ws[WS_Q + bh * DH + 8 * c + 2 * i]) | ((unsigned)f2bf(ws[WS_Q + bh * DH + 8 * c + 2 * i + 1]) << 16);
  const int per = SK / NSPLIT;
  const u16* kb = K + ((size_t)bh * SK + s * per) * DH;
  const u16* vb = V + ((size_t)bh * SK + s * per) * DH;
  float m = -1e30f, l = 0, acc[8] = {0, 0, 0, 0, 0, 0, 0, 0};
  const float C2 = 0.088388347648318440f * 1.4426950408889634f;
  for (int k0 = w * 16; k0 < per; k0 += 64) {
    v4u kk[4], vv[4];
    for (int i = 0; i < 4; ++i) { kk[i] = __builtin_nontemporal_load((const v4u*)(kb + (size_t)(k0 + 4 * i + r) * DH + 8 * c)); vv[i] = __builtin_nontemporal_load((const v4u*)(vb + (size_t)(k0 + 4 * i + r) * DH + 8 * c)); }
    float sc[4];
    for (int i = 0; i < 4; ++i) { float d = 0; for (int j = 0; j < 4; ++j) d = dot2(kk[i][j], qp[j], d); for (int o = 8; o; o >>= 1) d += __shfl_xor(d, o); sc[i] = rbf(rbf(d) * rbf(0.088388347648318440f)) * 1.4426950408889634f; }
    float mn = fmaxf(fmaxf(fmaxf(sc[0], sc[1]), fmaxf(sc[2], sc[3])), m);
    float al = exp2f(m - mn); m = mn; l *= al;
    for (int j = 0; j < 8; ++j) acc[j] *= al;
    for (int i = 0; i < 4; ++i) { float p = exp2f(sc[i] - m); l += p;
      for (int j = 0; j < 4; ++j) { acc[2 * j] += p * __uint_as_float(vv[i][j] << 16); acc[2 * j + 1] += p * __uint_as_float(vv[i][j] & 0xffff0000u); } }
  }
  int rr = w * 4 + r;
  for (int j = 0; j < 8; ++j) sm[rr][8 * c + j] = acc[j];
  if (c == 0) { sm[rr][128] = m; sm[rr][129] = l; }
  __syncthreads();
  if (t < 128) {
    float M = -1e30f; for (int i = 0; i < 16; ++i) M = fmaxf(M, sm[i][128]);
    float L = 0, o = 0; for (int i = 0; i < 16; ++i) { float wt = exp2f(sm[i][128] - M); L += wt * sm[i][129]; o += wt * sm[i][t]; }
    float* p = ws + WS_P + (size_t)blockIdx.x * 132;
    p[t] = o; if (t == 0) { p[128] = M; p[129] = L; }
  }
}
__global__ void k_comb(float* ws) {
  int bh = blockIdx.x, t = threadIdx.x;
  const float* p = ws + WS_P + (size_t)bh * NSPLIT * 132;
  float M = -1e30f; for (int i = 0; i < NSPLIT; ++i) M = fmaxf(M, p[i * 132 + 128]);
  float L = 0, o = 0; for (int i = 0; i < NSPLIT; ++i) { float wt = exp2f(p[i * 132 + 128] - M); L += wt * p[i * 132 + 129]; o += wt * p[i * 132 + t]; }
  ws[WS_O + bh * DH + t] = rbf(o / L);
}
__global__ void k_out(const u16* x, const u16* wo, float* ws, u16* out) {
  __shared__ float red[16][4][32];
  int c = blockIdx.x * 32 + (threadIdx.x & 31), kq = threadIdx.x >> 5;
  float a[B] = {0, 0, 0, 0};
  for (int k = kq * 256; k < kq * 256 + 256; ++k) { float w = bf2f(wo[k * D + c]); for (int b = 0; b < B; ++b) a[b] += ws[WS_O + b * H * DH + k] * w; }
  for (int b = 0; b < B; ++b) red[kq][b][threadIdx.x & 31] = a[b];
  __syncthreads();
  if (threadIdx.x < 128) { int b = threadIdx.x >> 5, cc = threadIdx.x & 31; float s = 0; for (int i = 0; i < 16; ++i) s += red[i][b][cc];
    int col = blockIdx.x * 32 + cc; s = rbf(s); out[b * D + col] = f2bf(bf2f(x[b * D + col]) + rbf(s * ws[WS_G + b * D + col])); }
}

extern "C" void kernel_launch(void* const* d_in, const int* in_sizes, int n_in, void* d_out, int out_size, void* d_ws, size_t ws_size, hipStream_t stream) {
  const u16* x = (const u16*)d_in[0]; const u16* K = (const u16*)d_in[1]; const u16* V = (const u16*)d_in[2];
  const u16* wq = (const u16*)d_in[3]; const u16* wo = (const u16*)d_in[4];
  const u16* wg = (const u16*)d_in[6]; const u16* wu = (const u16*)d_in[7]; const u16* wd = (const u16*)d_in[8];
  float* ws = (float*)d_ws;
  k_ln<<<B, 512, 0, stream>>>(x, ws);
  k_proj<<<(H * DH + F) / 64, 64, 0, stream>>>(wq, wg, wu, ws);
  k_down<<<D / 64, 64, 0, stream>>>(wd, ws);
  k_attn<<<B * H * NSPLIT, 256, 0, stream>>>(K, V, ws);
  k_comb<<<B * H, 128, 0, stream>>>(ws);
  k_out<<<D / 32, 512, 0, stream>>>(x, wo, ws, (u16*)d_out);
}
```

```cpp
#include <hip/hip_runtime.h>
#include <stdint.h>

typedef unsigned short u16;
typedef unsigned v4u __attribute__((ext_vector_type(4)));
typedef unsigned v2u __attribute__((ext_vector_type(2)));
typedef float v4f __attribute__((ext_vector_type(4)));
typedef float v2f __attribute__((ext_vector_type(2)));
typedef short v4s __attribute__((ext_vector_type(4)));
typedef __bf16 v2bf __attribute__((ext_vector_type(2)));
typedef __bf16 v8bf __attribute__((ext_vector_type(8)));
#define GAS __attribute__((address_space(1)))
#define LAS __attribute__((address_space(3)))

constexpr int INFL = 10;
constexpr int SIDE_G = 2;
constexpr int H_POLL_R = 26;
constexpr int G_POLL_R = 50;
constexpr int NT_AUX = 2, SC1_AUX = 16;
constexpr int NLOAD = 2, HELPER_WAVE = 2, CONS0 = 3;

constexpr int NB = 4, DM = 512, NH = 32, SK = 8192, DH = 128, FF = 1792;
constexpr int NWG = 256, NWAVE = 7, NTHR = NWAVE * 64, NT = 64;
constexpr int HROW = 512 + 16;
constexpr int L_RING = 0, L_W = 3 * 32768, L_WQ = 4 * 32768, L_H = L_W + 32 * 1024, L_XN = L_WQ + 16384, L_A = L_XN + 4096, AROW = 272, L_O = L_A + 4 * AROW, L_Q = L_O + 4 * 512, L_ML = L_Q + 256;
constexpr int L_DONE = L_ML + 32, L_LAND = L_DONE + 16, L_FLAG = L_LAND + 8, L_RDY = L_FLAG + 4, L_END = L_DONE + 32;
static_assert(L_H + NB * HROW <= L_XN && L_END <= 163840, "LDS map");
constexpr size_t WS_EPOCH = 0, WS_Q = 4096, WS_H = WS_Q + NH * NB * 2048, WS_G = WS_H + 2 * NB * FF * 4, WS_O = WS_G + NB * 64 * 512, WS_S = WS_O + NH * 8192, WS_Y = WS_S + NH * 128, WS_END = WS_Y + (size_t)NB * 64 * 2048;
constexpr float LOG2E = 1.4426950408889634f, SCALE = 0.088388347648318440f;
constexpr unsigned SPIN_CAP = 1u << 20;

__device__ __forceinline__ float asf(unsigned u) { return __builtin_bit_cast(float, u); }
__device__ __forceinline__ unsigned asu(float f) { return __builtin_bit_cast(unsigned, f); }
__device__ __forceinline__ unsigned pk_bf16(float lo, float hi) { const v2f v = {lo, hi}; return __builtin_bit_cast(unsigned, __builtin_convertvector(v, v2bf)); }
__device__ __forceinline__ float rbf(float f) { return asf(pk_bf16(f, f) << 16); }
__device__ __forceinline__ float bf_lo(unsigned u) { return asf(u << 16); }
__device__ __forceinline__ float bf_hi(unsigned u) { return asf(u & 0xffff0000u); }
__device__ __forceinline__ float dot2(unsigned a, unsigned b, float acc) { return __builtin_amdgcn_fdot2_f32_bf16(__builtin_bit_cast(v2bf, a), __builtin_bit_cast(v2bf, b), acc, false); }
template <int CTRL> __device__ __forceinline__ unsigned dppu(unsigned x) { return (unsigned)__builtin_amdgcn_mov_dpp((int)x, CTRL, 0xf, 0xf, true); }
template <int CTRL> __device__ __forceinline__ float dpp(float x) { return asf(dppu<CTRL>(asu(x))); }
constexpr int XOR1 = 0xB1, XOR2 = 0x4E, XOR7 = 0x141, ROR8 = 0x128, ROR4 = 0x124, BCAST = 0x150;
__device__ __forceinline__ float xrow_sum(float x) {
  auto s = __builtin_amdgcn_permlane16_swap(asu(x), asu(x), false, false); x = asf(s[0]) + asf(s[1]);
  auto t = __builtin_amdgcn_permlane32_swap(asu(x), asu(x), false, false); return asf(t[0]) + asf(t[1]);
}
__device__ __forceinline__ float xrow_max(float x) {
  auto s = __builtin_amdgcn_permlane16_swap(asu(x), asu(x), false, false); x = fmaxf(asf(s[0]), asf(s[1]));
  auto t = __builtin_amdgcn_permlane32_swap(asu(x), asu(x), false, false); return fmaxf(asf(t[0]), asf(t[1]));
}
__device__ __forceinline__ float wave_sum(float x) { x += dpp<XOR1>(x); x += dpp<XOR2>(x); x += dpp<XOR7>(x); x += dpp<ROR8>(x); return xrow_sum(x); }
__device__ __forceinline__ float silu(float x) { return x / (1.f + __expf(-x)); }
__device__ __forceinline__ float pick4(v4f d, int g) { const float a = (g & 1) ? d[1] : d[0], b = (g & 1) ? d[3] : d[2]; return (g & 2) ? b : a; }
__device__ __forceinline__ v4u tr_frag(LAS char* p0, LAS char* p1) {
  const v2u a = __builtin_bit_cast(v2u, __builtin_amdgcn_ds_read_tr16_b64_v4i16((LAS v4s*)p0));
  const v2u b = __builtin_bit_cast(v2u, __builtin_amdgcn_ds_read_tr16_b64_v4i16((LAS v4s*)p1));
  return v4u{a.x, a.y, b.x, b.y};
}
__device__ __forceinline__ v4f mfma(v4u a, v4u b, v4f c) { return __builtin_amdgcn_mfma_f32_16x16x32_bf16(__builtin_bit_cast(v8bf, a), __builtin_bit_cast(v8bf, b), c, 0, 0, 0); }
__device__ __forceinline__ v4u make_unit(float z0, float z1, int c, unsigned t0, unsigned t1) {
  const bool odd = c & 1;
  const float own = odd ? z1 : z0, rcv = dpp<XOR1>(odd ? z0 : z1);
  return v4u{asu(odd ? rcv : own), t0, asu(odd ? own : rcv), t1};
}
__device__ __forceinline__ int unit_idx(int c) { return ((c & 1) << 1) | ((c >> 1) & 1); }
__device__ __forceinline__ v4u make_unit_bf16(float z, unsigned tg) { const unsigned p = pk_bf16(z, dpp<XOR1>(z)); return v4u{p, tg, dppu<XOR2>(p), tg}; }
__device__ __forceinline__ v4u layernorm(v4u xr) {
  float v[8];
#pragma unroll
  for (int j = 0; j < 4; ++j) { v[2 * j] = bf_lo(xr[j]); v[2 * j + 1] = bf_hi(xr[j]); }
  const float s = ((v[0] + v[1]) + (v[2] + v[3])) + ((v[4] + v[5]) + (v[6] + v[7]));
  const float mean = wave_sum(s) * (1.f / DM);
  float qa = 0.f, qb = 0.f;
#pragma unroll
  for (int j = 0; j < 4; ++j) { const float d0 = v[2 * j] - mean, d1 = v[2 * j + 1] - mean; qa = fmaf(d0, d0, qa); qb = fmaf(d1, d1, qb); }
  const float var = rbf(wave_sum(qa + qb) * (1.f / DM)), mb = rbf(mean);
  const float den = rbf(sqrtf(rbf(var + 1e-5f)));
  float r = __builtin_amdgcn_rcpf(den); r = fmaf(fmaf(-den, r, 1.f), r, r);
  auto dv = [&](float n) { const float q = n * r; return fmaf(fmaf(-q, den, n), r, q); };
  v4u o;
#pragma unroll
  for (int j = 0; j < 4; ++j) o[j] = pk_bf16(dv(rbf(v[2 * j] - mb)), dv(rbf(v[2 * j + 1] - mb)));
  return o;
}
#define WG_BARRIER() do { asm volatile("s_waitcnt lgkmcnt(0)" ::: "memory"); __builtin_amdgcn_s_barrier(); asm volatile("" ::: "memory"); } while (0)
#define VM_DRAIN() do { __builtin_amdgcn_s_waitcnt(0x0f70); asm volatile("" ::: "memory"); } while (0)
#define RSRC(p, n) __builtin_amdgcn_make_buffer_rsrc((void*)(p), 0, (n), 0x00020000)
#define NEXT_SLOT(slot, t) ((t) == 2 ? 3u * 32768u : (slot) >= 2u * 32768u ? 0u : (slot) + 32768u)
#define LDSW(off) (*(volatile LAS unsigned*)(L + (off)))
#define WAIT_GE(off, v, nap) do { while (LDSW(off) < (unsigned)(v)) { if (nap) __builtin_amdgcn_s_sleep(nap); } asm volatile("" ::: "memory"); __builtin_amdgcn_sched_barrier(0); } while (0)
#define LDS_INC(off) do { if (lane == 0) asm volatile("s_waitcnt lgkmcnt(0)\n\tds_add_u32 %0, %1" :: "v"((unsigned)(__UINTPTR_TYPE__)(L + (off))), "v"(1u) : "memory"); } while (0)
#define SET_FLAG(v) do { asm volatile("s_waitcnt lgkmcnt(0)" ::: "memory"); if (lane == 0) LDSW(L_FLAG) = (v); } while (0)

__global__ void __launch_bounds__(NTHR, 2) fused_decode(const u16* __restrict__ x, const u16* __restrict__ kc, const u16* __restrict__ vc, const u16* __restrict__ wq,
                                                        const u16* __restrict__ wo, const u16* __restrict__ wgt, const u16* __restrict__ wup, const u16* __restrict__ wdn,
                                                        u16* __restrict__ out, char* __restrict__ ws) {
  extern __shared__ __attribute__((aligned(16))) char smem[];
  LAS char* L = (LAS char*)smem;
  asm volatile("" :: "s"(kc), "s"(vc), "s"(wq), "s"(x), "s"(ws));
  const int lane = threadIdx.x & 63, wave = __builtin_amdgcn_readfirstlane(threadIdx.x >> 6);
  const int wg = blockIdx.x, idx = wg >> 3;
  const int h = (wg & 1) | ((idx >> 1) << 1), sp = idx & 1, b = (wg >> 1) & 3;
  const int prod = h * 2 + sp;
  const int ks = 2 * b + sp;
  const int g = lane >> 4, c = lane & 15;
  if (wave >= NLOAD) {
  if (wave != HELPER_WAVE) {
    const int w = wave - CONS0;
    v4u xr = *(const GAS v4u*)(x + w * DM + lane * 8);
    __builtin_amdgcn_sched_barrier(0);
    __builtin_amdgcn_s_barrier();
    asm volatile("" : "+v"(xr) :: "memory");
    *(LAS v4u*)(L + L_XN + w * 1024 + lane * 16) = layernorm(xr);
    LDS_INC(L_RDY);
    WAIT_GE(L_FLAG, 1, 1);
    const v4u q = *(LAS v4u*)(L + L_Q + c * 16);
    float m = -INFINITY, l = 0.f;
    v2f acc[4] = {{0.f, 0.f}, {0.f, 0.f}, {0.f, 0.f}, {0.f, 0.f}};
    const bool c8 = c & 8, c4 = c & 4;
    unsigned slot = 0;
    LAS char* const lk = L + L_RING + w * 4096 + lane * 16;
#define PV2(k) do { const v2f p_ = {pt[k], pt[k]};                                                                  \
      _Pragma("unroll") for (int j = 0; j < 4; ++j) acc[j] = __builtin_elementwise_fma(p_, v2f{bf_lo(vb[k][j]), bf_hi(vb[k][j])}, acc[j]); } while (0)
#define ROUNDS(ta, tb, nap) _Pragma("unroll 1") for (int t = (ta); t < (tb); ++t) {                                  \
      v4u kb[4], vb[4];                                                                                              \
      WAIT_GE(L_LAND, t * 16 + 4 * w + 4, nap);                                                                      \
      _Pragma("unroll") for (int j = 0; j < 4; ++j) kb[j] = *(LAS v4u*)(lk + slot + j * 1024);                      \
      float d_[4];                                                                                                   \
      _Pragma("unroll") for (int j = 0; j < 4; ++j) d_[j] = dot2(kb[j].w, q.w, dot2(kb[j].z, q.z, dot2(kb[j].y, q.y, dot2(kb[j].x, q.x, 0.f)))); \
      const float e0 = (c8 ? d_[2] : d_[0]) + dpp<ROR8>(c8 ? d_[0] : d_[2]);                                        \
      const float e1 = (c8 ? d_[3] : d_[1]) + dpp<ROR8>(c8 ? d_[1] : d_[3]);                                        \
      float sc = (c4 ? e1 : e0) + dpp<XOR7>(c4 ? e0 : e1);                                                          \
      sc += dpp<XOR2>(sc); sc += dpp<XOR1>(sc);                                                                     \
      sc = rbf(rbf(sc) * SCALE);                                                                                     \
      float tm = fmaxf(sc, dpp<ROR4>(sc)); tm = fmaxf(tm, dpp<ROR8>(tm));                                           \
      if (__builtin_amdgcn_ballot_w64(tm > m)) {                                                                     \
        const float mn = fmaxf(m, tm), al = __builtin_amdgcn_exp2f((m - mn) * LOG2E);                               \
        m = mn; l *= al;                                                                                             \
        _Pragma("unroll") for (int j = 0; j < 4; ++j) acc[j] *= al;                                                 \
      }                                                                                                              \
      const float pe = __builtin_amdgcn_exp2f((sc - m) * LOG2E);                                                    \
      l += pe;                                                                                                       \
      float pt[4] = {dpp<BCAST + 0>(pe), dpp<BCAST + 4>(pe), dpp<BCAST + 8>(pe), dpp<BCAST + 12>(pe)};              \
      asm volatile("" : "+v"(pt[0]), "+v"(pt[1]), "+v"(pt[2]), "+v"(pt[3]));                                        \
      WAIT_GE(L_LAND + 4, t * 16 + 4 * w + 2, nap);                                                                  \
      vb[0] = *(LAS v4u*)(lk + slot + 16384); vb[1] = *(LAS v4u*)(lk + slot + 17408);                               \
      PV2(0); PV2(1);                                                                                                \
      asm volatile("" : "+v"(acc[0]), "+v"(acc[1]), "+v"(acc[2]), "+v"(acc[3]));                                    \
      WAIT_GE(L_LAND + 4, t * 16 + 4 * w + 4, nap);                                                                  \
      vb[2] = *(LAS v4u*)(lk + slot + 18432); vb[3] = *(LAS v4u*)(lk + slot + 19456);                               \
      asm volatile("s_waitcnt lgkmcnt(0)" : "+v"(vb[2]), "+v"(vb[3]) :: "memory");                                  \
      if (lane == 0) LDSW(L_DONE + w * 4) = t + 1;                                                                   \
      __builtin_amdgcn_sched_barrier(0);                                                                             \
      PV2(2); PV2(3);                                                                                                \
      slot = NEXT_SLOT(slot, t);                                                                                     \
    }
    ROUNDS(0, NT, 1)
    {
      const float mw = xrow_max(m), f = __builtin_amdgcn_exp2f((m - mw) * LOG2E);
      float lw = l * f; lw += dpp<ROR4>(lw); lw += dpp<ROR8>(lw); lw = xrow_sum(lw);
      float u[4], v[2];
#pragma unroll
      for (int j = 0; j < 4; ++j) { auto sw = __builtin_amdgcn_permlane16_swap(asu(acc[j >> 1][j & 1] * f), asu(acc[2 + (j >> 1)][j & 1] * f), false, false); u[j] = asf(sw[0]) + asf(sw[1]); }
#pragma unroll
      for (int j = 0; j < 2; ++j) { auto sw = __builtin_amdgcn_permlane32_swap(asu(u[j]), asu(u[j + 2]), false, false); v[j] = asf(sw[0]) + asf(sw[1]); }
      *(LAS v2f*)(L + L_O + w * 512 + c * 32 + (g & 1) * 16 + (g & 2) * 4) = v2f{v[0], v[1]};
      if (lane == 0) *(LAS v2f*)(L + L_ML + w * 8) = v2f{mw, lw};
    }
    WG_BARRIER();
  } else {
    __builtin_amdgcn_s_setprio(2);
    unsigned tg = *(const GAS unsigned*)(ws + WS_EPOCH);
    unsigned xw = *(const GAS unsigned*)(x + b * DM + prod * 8 + (lane & 3) * 2);
    __builtin_amdgcn_sched_barrier(0);
    __builtin_amdgcn_s_barrier();
    VM_DRAIN();
    asm volatile("" : "+v"(xw), "+v"(tg));
    tg += 1u;
    const v4f z4 = {0.f, 0.f, 0.f, 0.f};
    LAS char* const a_xn = L + L_XN + (lane & 3) * 1024 + g * 16;
    const int trow = 8 * g + (c >> 2);
    unsigned spins = 0;
#define POLL_FAILED(bad) (__builtin_amdgcn_ballot_w64(bad) != 0 && ++spins < SPIN_CAP)
    {
      WAIT_GE(L_RDY, 6, 1);
      LAS char* b0 = L + L_WQ + trow * 64 + 16 * (c & 3);
      v4u fa[8], fb[8][2];
#pragma unroll
      for (int i = 0; i < 8; ++i) { fa[i] = *(LAS v4u*)(a_xn + (b >> 1) * 512 + i * 64); fb[i][0] = tr_frag(b0 + i * 2048, b0 + i * 2048 + 256); fb[i][1] = tr_frag(b0 + i * 2048 + 8, b0 + i * 2048 + 264); }
      __builtin_amdgcn_sched_barrier(0);
      v4f d0 = z4, d1 = z4;
#pragma unroll
      for (int i = 0; i < 8; ++i) { d0 = mfma(fa[i], fb[i][0], d0); d1 = mfma(fa[i], fb[i][1], d1); }
      const auto rq = RSRC(ws + WS_Q + h * NB * 2048, NB * 2048);
      __builtin_amdgcn_raw_buffer_store_b128(make_unit(pick4(d0, g), pick4(d1, g), c, tg, tg), rq, g * 2048 + (b >> 1) * 1024 + ((b & 1) * 2 + sp) * 256 + (c >> 2) * 64 + unit_idx(c) * 16, 0, SC1_AUX);
      __builtin_amdgcn_s_sleep(20);
      v4u u0, u1;
      do {
        asm volatile("" ::: "memory");
        u0 = __builtin_amdgcn_raw_buffer_load_b128(rq, b * 2048 + lane * 16, 0, SC1_AUX); u1 = __builtin_amdgcn_raw_buffer_load_b128(rq, b * 2048 + 1024 + lane * 16, 0, SC1_AUX);
      } while (POLL_FAILED(((u0.y ^ tg) | (u0.w ^ tg) | (u1.y ^ tg) | (u1.w ^ tg)) != 0));
      *(LAS unsigned*)(L + L_Q + lane * 4) = pk_bf16(asf(u0.x) + asf(u1.x), asf(u0.z) + asf(u1.z));
      SET_FLAG(1u);
    }
    while (min(min(LDSW(L_DONE), LDSW(L_DONE + 4)), min(LDSW(L_DONE + 8), LDSW(L_DONE + 12))) < 4u) __builtin_amdgcn_s_sleep(4);
    asm volatile("" ::: "memory");
#define SIDE_LOAD(SG, rs, n, ldsstep, voff, goff, gstep, aux) do { _Pragma("unroll 1") for (int i0 = 0; i0 < (n); i0 += (SG)) { \
        _Pragma("unroll") for (int i = 0; i < (SG); ++i) __builtin_amdgcn_raw_ptr_buffer_load_lds(rs, (LAS void*)(L + L_W + (i0 + i) * (ldsstep)), 16, voff, (goff) + (i0 + i) * (gstep), 0, aux); \
        asm volatile("s_waitcnt vmcnt(%0)" :: "i"(SG) : "memory"); } VM_DRAIN(); } while (0)
    if (idx >= 1 && idx < 8) {
      const int fi = (wg & 7) * 7 + idx - 1, mat = fi & 1, col0 = (fi >> 1) * 64;
      const unsigned vf = (lane >> 3) * (FF * 2) + (lane & 7) * 16;
      const auto rw = RSRC((mat ? wup : wgt) + col0, (DM * FF - col0) * 2);
      v4f dg[4] = {z4, z4, z4, z4};
      LAS char* b0 = L + L_W + trow * 128 + 8 * (c & 3);
#pragma unroll 1
      for (int half = 0; half < 2; ++half) {
        SIDE_LOAD(SIDE_G, rw, 32, 1024, vf, half * 256 * FF * 2, 8 * FF * 2, NT_AUX);
#pragma unroll 2
        for (int i = 0; i < 8; ++i) {
          const v4u a = *(LAS v4u*)(a_xn + (half * 8 + i) * 64);
#pragma unroll
          for (int n = 0; n < 4; ++n) dg[n] = mfma(a, tr_frag(b0 + i * 4096 + 32 * n, b0 + i * 4096 + 512 + 32 * n), dg[n]);
        }
        asm volatile("s_waitcnt lgkmcnt(0)" : "+v"(dg[0]), "+v"(dg[1]), "+v"(dg[2]), "+v"(dg[3]) :: "memory");
      }
      const auto rh = RSRC(ws + WS_H, 2 * NB * FF * 4);
#pragma unroll
      for (int n = 0; n < 4; ++n) {
        const v4u unit = make_unit_bf16(pick4(dg[n], g), tg);
        if ((c & 3) == 0) __builtin_amdgcn_raw_buffer_store_b128(unit, rh, (mat * NB + g) * (FF * 4) + (col0 + 16 * n + c) * 4, 0, SC1_AUX);
      }
    } else if (idx >= 8 && idx < 15) {
      const int col0 = (wg & 7) * 64, rg = idx - 8;
      const auto rd = RSRC(wdn + (size_t)rg * 256 * DM + col0, (256 * DM - col0) * 2);
      SIDE_LOAD(SIDE_G, rd, 32, 1024, (lane >> 3) * (DM * 2) + (lane & 7) * 16, 0, 8 * DM * 2, NT_AUX);
      WAIT_GE(L_LAND, H_POLL_R * 16, 8);
      const auto rh = RSRC(ws + WS_H, 2 * NB * FF * 4);
      v4u u[8]; unsigned bad;
      do {
        asm volatile("" ::: "memory");
#pragma unroll
        for (int i = 0; i < 8; ++i) u[i] = __builtin_amdgcn_raw_buffer_load_b128(rh, rg * 1024 + lane * 16, i * (FF * 4), SC1_AUX);
        bad = 0;
#pragma unroll
        for (int i = 0; i < 8; ++i) bad |= (u[i].y ^ tg) | (u[i].w ^ tg);
        if (__builtin_amdgcn_ballot_w64(bad != 0)) __builtin_amdgcn_s_sleep(100);
      } while (POLL_FAILED(bad != 0));
#pragma unroll
      for (int r = 0; r < NB; ++r) {
        const v4u gq = u[r], uq = u[NB + r];
        *(LAS v2u*)(L + L_H + r * HROW + lane * 8) = v2u{pk_bf16(rbf(silu(bf_lo(gq.x))) * bf_lo(uq.x), rbf(silu(bf_hi(gq.x))) * bf_hi(uq.x)),
                                                         pk_bf16(rbf(silu(bf_lo(gq.z))) * bf_lo(uq.z), rbf(silu(bf_hi(gq.z))) * bf_hi(uq.z))};
      }
      LAS char* a0 = L + L_H + (lane & 3) * HROW + g * 16;
      LAS char* b0 = L + L_W + trow * 128 + 16 * (c & 3);
      v4f d[2][2] = {{z4, z4}, {z4, z4}};
#pragma unroll 2
      for (int i = 0; i < 8; ++i) {
        const v4u a = *(LAS v4u*)(a0 + i * 64);
#pragma unroll
        for (int qq = 0; qq < 2; ++qq)
#pragma unroll
          for (int e = 0; e < 2; ++e) d[qq][e] = mfma(a, tr_frag(b0 + i * 4096 + 64 * qq + 8 * e, b0 + i * 4096 + 512 + 64 * qq + 8 * e), d[qq][e]);
      }
      const auto rf = RSRC(ws + WS_G, NB * 64 * 512);
#pragma unroll
      for (int qq = 0; qq < 2; ++qq)
        __builtin_amdgcn_raw_buffer_store_b128(make_unit(pick4(d[qq][0], g), pick4(d[qq][1], g), c, tg, tg), rf, (g * 64 + (col0 >> 3) + 4 * qq + (c >> 2)) * 512 + rg * 64 + unit_idx(c) * 16, 0, SC1_AUX);
      asm volatile("s_waitcnt lgkmcnt(0)" ::: "memory");
    }
    float gate0, gate1;
    {
      const auto rf = RSRC(ws + WS_G + (b * 64 + prod) * 512, 512);
      WAIT_GE(L_LAND, G_POLL_R * 16, 8);
      const bool on = lane < 28;
      v4u u;
      do { asm volatile("" ::: "memory"); u = __builtin_amdgcn_raw_buffer_load_b128(rf, (lane & 31) * 16, 0, SC1_AUX); } while (POLL_FAILED(on && (u.y != tg || u.w != tg)));
      float f0 = on ? asf(u.x) : 0.f, f1 = on ? asf(u.z) : 0.f;
      f0 += dpp<ROR4>(f0); f0 += dpp<ROR8>(f0); f0 = xrow_sum(f0);
      f1 += dpp<ROR4>(f1); f1 += dpp<ROR8>(f1); f1 = xrow_sum(f1);
      gate0 = rbf(silu(rbf(f0))); gate1 = rbf(silu(rbf(f1)));
      asm volatile("" : "+v"(gate0), "+v"(gate1));
    }
    {
      const auto ro = RSRC(wo + (size_t)h * DH * DM + ks * 64, (DH * DM - ks * 64) * 2);
      WAIT_GE(L_LAND, NT * 16 - 12, 2);
#pragma unroll 1
      for (int i = 0; i < 16; ++i) __builtin_amdgcn_raw_ptr_buffer_load_lds(ro, (LAS void*)(L + L_W + i * 1024), 16, (lane >> 3) * (DM * 2) + (lane & 7) * 16, i * 8 * DM * 2, 0, NT_AUX);
    }
    WG_BARRIER();
    {
      const auto ro = RSRC(ws + WS_O, NH * (8192 + 128));
      {
        const v4f ml0 = *(LAS v4f*)(L + L_ML), ml1 = *(LAS v4f*)(L + L_ML + 16);
        v2f p[4];
#pragma unroll
        for (int i = 0; i < 4; ++i) p[i] = *(LAS v2f*)(L + L_O + i * 512 + lane * 8);
        const float M = fmaxf(fmaxf(ml0[0], ml0[2]), fmaxf(ml1[0], ml1[2]));
        const float c0 = __builtin_amdgcn_exp2f((ml0[0] - M) * LOG2E), c1 = __builtin_amdgcn_exp2f((ml0[2] - M) * LOG2E);
        const float c2 = __builtin_amdgcn_exp2f((ml1[0] - M) * LOG2E), c3 = __builtin_amdgcn_exp2f((ml1[2] - M) * LOG2E);
        const float Ls = fmaf(c0, ml0[1], c1 * ml0[3]) + fmaf(c2, ml1[1], c3 * ml1[3]);
        float inv = __builtin_amdgcn_rcpf(Ls); inv = fmaf(fmaf(-Ls, inv, 1.f), inv, inv);
        const float o0 = (fmaf(c0, p[0][0], c1 * p[1][0]) + fmaf(c2, p[2][0], c3 * p[3][0])) * inv;
        const float o1 = (fmaf(c0, p[0][1], c1 * p[1][1]) + fmaf(c2, p[2][1], c3 * p[3][1])) * inv;
        __builtin_amdgcn_raw_buffer_store_b128(v4u{asu(o0), tg, asu(o1), tg}, ro, ks * 1024 + lane * 16, h * 8192, SC1_AUX);
        const unsigned lu = asu(fmaf(M, LOG2E, __builtin_amdgcn_logf(Ls)));
        if (lane == 0) __builtin_amdgcn_raw_buffer_store_b128(v4u{lu, tg, lu, tg}, ro, ks * 16, NH * 8192 + h * 128, SC1_AUX);
      }
      v4u wf[4][2][2];
      {
        asm volatile("s_waitcnt vmcnt(2)" ::: "memory");
        LAS char* b0 = L + L_W + trow * 128 + 16 * (c & 3);
        const unsigned ab = (unsigned)(__UINTPTR_TYPE__)b0;
#define TRQ(i, qq, e) asm volatile("ds_read_b64_tr_b16 %0, %2 offset:%3\n\tds_read_b64_tr_b16 %1, %2 offset:%4" : "=&v"(lo_[(i) * 4 + (qq) * 2 + (e)]), "=&v"(hi_[(i) * 4 + (qq) * 2 + (e)]) \
          : "v"(ab), "i"((i) * 4096 + 64 * (qq) + 8 * (e)), "i"((i) * 4096 + 512 + 64 * (qq) + 8 * (e)) : "memory")
        v2u lo_[16], hi_[16];
#define TRQ_WAIT(o) asm volatile("s_waitcnt lgkmcnt(0)" : "+v"(lo_[o]), "+v"(lo_[o + 1]), "+v"(lo_[o + 2]), "+v"(lo_[o + 3]), "+v"(lo_[o + 4]), "+v"(lo_[o + 5]), "+v"(lo_[o + 6]), "+v"(lo_[o + 7]), \
          "+v"(hi_[o]), "+v"(hi_[o + 1]), "+v"(hi_[o + 2]), "+v"(hi_[o + 3]), "+v"(hi_[o + 4]), "+v"(hi_[o + 5]), "+v"(hi_[o + 6]), "+v"(hi_[o + 7]) :: "memory")
        TRQ(0, 0, 0); TRQ(0, 0, 1); TRQ(0, 1, 0); TRQ(0, 1, 1); TRQ(1, 0, 0); TRQ(1, 0, 1); TRQ(1, 1, 0); TRQ(1, 1, 1); TRQ_WAIT(0);
        TRQ(2, 0, 0); TRQ(2, 0, 1); TRQ(2, 1, 0); TRQ(2, 1, 1); TRQ(3, 0, 0); TRQ(3, 0, 1); TRQ(3, 1, 0); TRQ(3, 1, 1); TRQ_WAIT(8);
#pragma unroll
        for (int i = 0; i < 16; ++i) wf[i >> 2][(i >> 1) & 1][i & 1] = v4u{lo_[i].x, lo_[i].y, hi_[i].x, hi_[i].y};
      }
      v4u u[8], us; unsigned miss = 0x1ffu;
      for (;;) {
        asm volatile("" ::: "memory");
        if (miss & 0x100u) us = __builtin_amdgcn_raw_buffer_load_b128(ro, (lane & 7) * 16, NH * 8192 + h * 128, SC1_AUX);
#pragma unroll
        for (int i = 0; i < 8; ++i) if (miss & (1u << i)) u[i] = __builtin_amdgcn_raw_buffer_load_b128(ro, lane * 16 + (i & 3) * 1024, h * 8192 + (i >> 2) * 4096, SC1_AUX);
        unsigned bd[9], bad;
        bad = bd[8] = (us.y ^ tg) | (us.w ^ tg);
#pragma unroll
        for (int i = 0; i < 8; ++i) bad |= bd[i] = (u[i].y ^ tg) | (u[i].w ^ tg);
        if (!POLL_FAILED(bad != 0)) break;
        miss = 0;
#pragma unroll
        for (int i = 0; i < 9; ++i) miss |= __builtin_amdgcn_ballot_w64(bd[i] != 0) ? 1u << i : 0u;
      }
      const unsigned wl = asu(__builtin_amdgcn_rcpf(1.f + __builtin_amdgcn_exp2f(dpp<XOR1>(asf(us.x)) - asf(us.x))));
#pragma unroll
      for (int r = 0; r < NB; ++r) {
        const float wa = asf(__builtin_amdgcn_readlane(wl, 2 * r)), wb = asf(__builtin_amdgcn_readlane(wl, 2 * r + 1));
        *(LAS unsigned*)(L + L_A + r * AROW + lane * 4) = pk_bf16(fmaf(wa, asf(u[2 * r].x), wb * asf(u[2 * r + 1].x)), fmaf(wa, asf(u[2 * r].z), wb * asf(u[2 * r + 1].z)));
      }
      v4f d[2][2] = {{z4, z4}, {z4, z4}};
#pragma unroll
      for (int i = 0; i < 4; ++i) {
        const v4u a = *(LAS v4u*)(L + L_A + (lane & 3) * AROW + i * 64 + g * 16);
#pragma unroll
        for (int qq = 0; qq < 2; ++qq)
#pragma unroll
          for (int e = 0; e < 2; ++e) d[qq][e] = mfma(a, wf[i][qq][e], d[qq][e]);
      }
      const auto ry = RSRC(ws + WS_Y, NB * 64 * 2048);
#pragma unroll
      for (int qq = 0; qq < 2; ++qq)
        __builtin_amdgcn_raw_buffer_store_b128(make_unit(pick4(d[qq][0], g), pick4(d[qq][1], g), c, tg, tg), ry, (g * 64 + ks * 8 + 4 * qq + (c >> 2)) * 2048 + h * 64 + unit_idx(c) * 16, 0, SC1_AUX);
      v4u y0, y1;
      do {
        asm volatile("" ::: "memory");
        y0 = __builtin_amdgcn_raw_buffer_load_b128(ry, lane * 16, (b * 64 + prod) * 2048, SC1_AUX); y1 = __builtin_amdgcn_raw_buffer_load_b128(ry, lane * 16 + 1024, (b * 64 + prod) * 2048, SC1_AUX);
      } while (POLL_FAILED(((y0.y ^ tg) | (y0.w ^ tg) | (y1.y ^ tg) | (y1.w ^ tg)) != 0));
      float s0 = asf(y0.x) + asf(y1.x), s1 = asf(y0.z) + asf(y1.z);
      s0 += dpp<ROR4>(s0); s0 += dpp<ROR8>(s0); s0 = xrow_sum(s0);
      s1 += dpp<ROR4>(s1); s1 += dpp<ROR8>(s1); s1 = xrow_sum(s1);
      const unsigned o = pk_bf16(bf_lo(xw) + rbf(rbf(s0) * gate0), bf_hi(xw) + rbf(rbf(s1) * gate1));
      if (lane < 4) *(GAS unsigned*)(out + b * DM + prod * 8 + lane * 2) = o;
      if (wg == 0 && lane == 0) __hip_atomic_store((GAS unsigned*)(ws + WS_EPOCH), tg, __ATOMIC_RELAXED, __HIP_MEMORY_SCOPE_AGENT);
    }
  }
  } else {
    __builtin_amdgcn_s_setprio(3);
    const unsigned vo = lane * 16;
    const u16* const sbase = (wave ? vc : kc) + (size_t)(b * NH + h) * SK * DH;
    const auto rs = RSRC(sbase, SK * DH * 2);
    unsigned so = sp * 16384, lo = L_RING + wave * 16384;
#define LD_PAIR() do { __builtin_amdgcn_raw_ptr_buffer_load_lds(rs, (LAS void*)(L + lo), 16, vo, so, 0, NT_AUX); \
      __builtin_amdgcn_raw_ptr_buffer_load_lds(rs, (LAS void*)(L + lo), 16, vo, so, 1024, NT_AUX); so += 2048; lo += 2048; } while (0)
    LD_PAIR();
    {
      const int off = (b >> 1) * 256 * NH * DH + h * DH + (b & 1) * 64 + sp * 32;
      const auto rq = RSRC(wq + off, (DM * NH * DH - off) * 2);
      const unsigned v0 = (lane >> 2) * 8192 + (lane & 3) * 16;
#pragma unroll 1
      for (int i = wave * 8; i < wave * 8 + 8; ++i) __builtin_amdgcn_raw_ptr_buffer_load_lds(rq, (LAS void*)(L + L_WQ + i * 1024), 16, v0, i * 16 * 8192, 0, NT_AUX);
    }
    constexpr int PRE = INFL / 2;
#pragma unroll 1
    for (int n = 1; n < PRE; ++n) LD_PAIR();
    if (wave == 0 && lane < 8) LDSW(L_DONE + lane * 4) = 0u;
    WG_BARRIER();
    asm volatile("s_waitcnt vmcnt(%0)" :: "i"(2 * PRE - 2) : "memory");
    LDS_INC(L_RDY);
    const unsigned a_done = (unsigned)(__UINTPTR_TYPE__)(L + L_DONE);
    volatile LAS unsigned* const land = (volatile LAS unsigned*)(L + L_LAND + wave * 4);
    unsigned lfloor = 0;
#define LANDED(n) do { lfloor = max((unsigned)(n), lfloor); if (lane == 0) *land = lfloor; } while (0)
    const v4u rsv = {(unsigned)(__UINTPTR_TYPE__)sbase, (unsigned)((__UINTPTR_TYPE__)sbase >> 32) & 0xffffu, SK * DH * 2, 0x00020000u};
    const unsigned a_land = (unsigned)(__UINTPTR_TYPE__)land;
    int cnt = 2 * PRE + 1 - INFL, left = 8 - PRE;
    const unsigned l0 = (unsigned)(__UINTPTR_TYPE__)L + wave * 16384;
    unsigned slot = 0;
    lo += (unsigned)(__UINTPTR_TYPE__)L;
#pragma unroll 1
    for (int t = 0; t < NT; ++t) {
      v4u dn; unsigned vt, pub, t0_, t1_, t2_, t3_;
      asm volatile(
          "s_mov_b32 m0, %[lo0]\n\t"
          "s_mov_b32 %[lo], %[lo0]\n\t"
          "s_mov_b32 %[so], %[so0]\n\t"
          "s_mov_b32 %[cnt], %[cnt0]\n\t"
          "s_mov_b32 %[left], %[left0]\n"
          "1:\n\t"
          "s_waitcnt vmcnt(%[w])\n\t"
          "buffer_load_dwordx4 %[vo], %[rs], %[so] offen nt lds\n\t"
          "s_max_i32 %[pub], %[cnt], %[fl]\n\t"
          "v_mov_b32 %[vt], %[pub]\n\t"
          "s_mov_b64 exec, 1\n\t"
          "ds_write_b32 %[al], %[vt]\n\t"
          "s_mov_b64 exec, -1\n\t"
          "ds_read_b128 %[dn], %[ad]\n\t"
          "s_add_i32 %[cnt], %[cnt], 2\n\t"
          "s_add_i32 %[lo], %[lo], 0x800\n\t"
          "s_waitcnt vmcnt(%[w])\n\t"
          "buffer_load_dwordx4 %[vo], %[rs], %[so] offen offset:1024 nt lds\n\t"
          "s_add_i32 %[so], %[so], 0x800\n\t"
          "s_mov_b32 m0, %[lo]\n\t"
          "s_sub_i32 %[left], %[left], 1\n\t"
          "s_cmp_lg_u32 %[left], 0\n\t"
          "s_cbranch_scc1 1b\n\t"
          "s_waitcnt lgkmcnt(0)"
          : [lo] "=&s"(t0_), [so] "=&s"(t1_), [cnt] "=&s"(t2_), [left] "=&s"(t3_), [pub] "=&s"(pub), [vt] "=&v"(vt), [dn] "=&v"(dn)
          : [lo0] "s"(lo), [so0] "s"(so), [cnt0] "s"(cnt), [left0] "s"(left), [vo] "v"(vo), [rs] "s"(rsv), [fl] "s"(lfloor), [al] "v"(a_land), [ad] "v"(a_done), [w] "i"(INFL - 1)
          : "memory", "scc");
      left = 8; cnt = 16 * t + 17 - INFL; so = sp * 16384 + (t + 1) * 32768;
      slot = NEXT_SLOT(slot, t); lo = l0 + slot;
      {
        const unsigned need = t == NT - 1 ? 0 : max(t - (t < 6 ? 2 : 1), 0);
        while (__builtin_amdgcn_readfirstlane(min(min(dn.x, dn.y), min(dn.z, dn.w))) < need) {
          asm volatile("s_waitcnt vmcnt(0)" ::: "memory");
          LANDED(t * 16 + 16);
          asm volatile("ds_read_b128 %0, %1\n\ts_waitcnt lgkmcnt(0)" : "=v"(dn) : "v"(a_done) : "memory");
        }
      }
    }
#pragma unroll
    for (int r = (INFL - 1) / 2; r >= 0; --r) { asm volatile("s_waitcnt vmcnt(%0)" :: "i"(2 * r) : "memory"); LANDED(NT * 16 - 2 * r); }
    WG_BARRIER();
  }
}

extern "C" void kernel_launch(void* const* d_in, const int* in_sizes, int n_in, void* d_out, int out_size, void* d_ws, size_t ws_size, hipStream_t stream) {
  (void)in_sizes; (void)n_in; (void)out_size;
  if (ws_size < WS_END) return;
  (void)hipFuncSetAttribute((const void*)fused_decode, hipFuncAttributeMaxDynamicSharedMemorySize, L_END);
  fused_decode<<<NWG, NTHR, L_END, stream>>>((const u16*)d_in[0], (const u16*)d_in[1], (const u16*)d_in[2], (const u16*)d_in[3], (const u16*)d_in[4],
                                             (const u16*)d_in[6], (const u16*)d_in[7], (const u16*)d_in[8], (u16*)d_out, (char*)d_ws);
}
```

```cpp
#include <hip/hip_runtime.h>
#include <stdint.h>

typedef unsigned short u16;
typedef unsigned v4u __attribute__((ext_vector_type(4)));
typedef unsigned v2u __attribute__((ext_vector_type(2)));
typedef float v4f __attribute__((ext_vector_type(4)));
typedef float v2f __attribute__((ext_vector_type(2)));
typedef short v4s __attribute__((ext_vector_type(4)));
typedef __bf16 v2bf __attribute__((ext_vector_type(2)));
typedef __bf16 v8bf __attribute__((ext_vector_type(8)));
#define GAS __attribute__((address_space(1)))
#define LAS __attribute__((address_space(3)))

constexpr int INFL = 10;
constexpr int W1 = 11, W2 = 12, W3 = 14;
constexpr int LAG1 = 30, LAG2 = 120, LAG3 = 300;
constexpr int T_S = 250, TAU_Q8S = 28544;
constexpr int SIDE_G = 2;
constexpr int H_POLL_R = 26;
constexpr int G_POLL_R = 50;
constexpr int NT_AUX = 2, SC1_AUX = 16;
constexpr int NLOAD = 2, HELPER_WAVE = 2, CONS0 = 3;

constexpr int NB = 4, DM = 512, NH = 32, SK = 8192, DH = 128, FF = 1792;
constexpr int NWG = 256, NWAVE = 7, NTHR = NWAVE * 64, NT = 64;
constexpr int HROW = 512 + 16;
constexpr int L_RING = 0, L_W = 3 * 32768, L_WQ = 4 * 32768, L_H = L_W + 32 * 1024, L_XN = L_WQ + 16384, L_A = L_XN + 4096, AROW = 272, L_O = L_A + 4 * AROW, L_Q = L_O + 4 * 512, L_ML = L_Q + 256;
constexpr int L_DONE = L_ML + 32, L_LAND = L_DONE + 16, L_FLAG = L_LAND + 8, L_RDY = L_FLAG + 4, L_OROW = L_RDY + 4, L_TAG = L_OROW + 4, L_END = L_DONE + 48;
static_assert(L_H + NB * HROW <= L_XN && L_END <= 163840, "LDS map");
constexpr size_t WS_EPOCH = 0, WS_Q = 4096, WS_H = WS_Q + NH * NB * 2048, WS_G = WS_H + 2 * NB * FF * 4, WS_O = WS_G + NB * 64 * 512, WS_S = WS_O + NH * 8192, WS_Y = WS_S + NH * 128, WS_END = WS_Y + (size_t)NB * 64 * 2048;
constexpr float LOG2E = 1.4426950408889634f, SCALE = 0.088388347648318440f;
constexpr unsigned SPIN_CAP = 1u << 20;

__device__ __forceinline__ float asf(unsigned u) { return __builtin_bit_cast(float, u); }
__device__ __forceinline__ unsigned asu(float f) { return __builtin_bit_cast(unsigned, f); }
__device__ __forceinline__ unsigned pk_bf16(float lo, float hi) { const v2f v = {lo, hi}; return __builtin_bit_cast(unsigned, __builtin_convertvector(v, v2bf)); }
__device__ __forceinline__ float rbf(float f) { return asf(pk_bf16(f, f) << 16); }
__device__ __forceinline__ float bf_lo(unsigned u) { return asf(u << 16); }
__device__ __forceinline__ float bf_hi(unsigned u) { return asf(u & 0xffff0000u); }
__device__ __forceinline__ float dot2(unsigned a, unsigned b, float acc) { return __builtin_amdgcn_fdot2_f32_bf16(__builtin_bit_cast(v2bf, a), __builtin_bit_cast(v2bf, b), acc, false); }
template <int CTRL> __device__ __forceinline__ unsigned dppu(unsigned x) { return (unsigned)__builtin_amdgcn_mov_dpp((int)x, CTRL, 0xf, 0xf, true); }
template <int CTRL> __device__ __forceinline__ float dpp(float x) { return asf(dppu<CTRL>(asu(x))); }
constexpr int XOR1 = 0xB1, XOR2 = 0x4E, XOR7 = 0x141, ROR8 = 0x128, ROR4 = 0x124, BCAST = 0x150;
__device__ __forceinline__ float xrow_sum(float x) {
  auto s = __builtin_amdgcn_permlane16_swap(asu(x), asu(x), false, false); x = asf(s[0]) + asf(s[1]);
  auto t = __builtin_amdgcn_permlane32_swap(asu(x), asu(x), false, false); return asf(t[0]) + asf(t[1]);
}
__device__ __forceinline__ float xrow_max(float x) {
  auto s = __builtin_amdgcn_permlane16_swap(asu(x), asu(x), false, false); x = fmaxf(asf(s[0]), asf(s[1]));
  auto t = __builtin_amdgcn_permlane32_swap(asu(x), asu(x), false, false); return fmaxf(asf(t[0]), asf(t[1]));
}
__device__ __forceinline__ float wave_sum(float x) { x += dpp<XOR1>(x); x += dpp<XOR2>(x); x += dpp<XOR7>(x); x += dpp<ROR8>(x); return xrow_sum(x); }
__device__ __forceinline__ float silu(float x) { return x / (1.f + __expf(-x)); }
__device__ __forceinline__ float pick4(v4f d, int g) { const float a = (g & 1) ? d[1] : d[0], b = (g & 1) ? d[3] : d[2]; return (g & 2) ? b : a; }
__device__ __forceinline__ v4u tr_frag(LAS char* p0, LAS char* p1) {
  const v2u a = __builtin_bit_cast(v2u, __builtin_amdgcn_ds_read_tr16_b64_v4i16((LAS v4s*)p0));
  const v2u b = __builtin_bit_cast(v2u, __builtin_amdgcn_ds_read_tr16_b64_v4i16((LAS v4s*)p1));
  return v4u{a.x, a.y, b.x, b.y};
}
__device__ __forceinline__ v4f mfma(v4u a, v4u b, v4f c) { return __builtin_amdgcn_mfma_f32_16x16x32_bf16(__builtin_bit_cast(v8bf, a), __builtin_bit_cast(v8bf, b), c, 0, 0, 0); }
__device__ __forceinline__ v4u make_unit(float z0, float z1, int c, unsigned t0, unsigned t1) {
  const bool odd = c & 1;
  const float own = odd ? z1 : z0, rcv = dpp<XOR1>(odd ? z0 : z1);
  return v4u{asu(odd ? rcv : own), t0, asu(odd ? own : rcv), t1};
}
__device__ __forceinline__ int unit_idx(int c) { return ((c & 1) << 1) | ((c >> 1) & 1); }
__device__ __forceinline__ v4u make_unit_bf16(float z, unsigned tg) { const unsigned p = pk_bf16(z, dpp<XOR1>(z)); return v4u{p, tg, dppu<XOR2>(p), tg}; }
__device__ __forceinline__ v4u layernorm(v4u xr) {
  float v[8];
#pragma unroll
  for (int j = 0; j < 4; ++j) { v[2 * j] = bf_lo(xr[j]); v[2 * j + 1] = bf_hi(xr[j]); }
  const float s = ((v[0] + v[1]) + (v[2] + v[3])) + ((v[4] + v[5]) + (v[6] + v[7]));
  const float mean = wave_sum(s) * (1.f / DM);
  float qa = 0.f, qb = 0.f;
#pragma unroll
  for (int j = 0; j < 4; ++j) { const float d0 = v[2 * j] - mean, d1 = v[2 * j + 1] - mean; qa = fmaf(d0, d0, qa); qb = fmaf(d1, d1, qb); }
  const float var = rbf(wave_sum(qa + qb) * (1.f / DM)), mb = rbf(mean);
  const float den = rbf(sqrtf(rbf(var + 1e-5f)));
  float r = __builtin_amdgcn_rcpf(den); r = fmaf(fmaf(-den, r, 1.f), r, r);
  auto dv = [&](float n) { const float q = n * r; return fmaf(fmaf(-q, den, n), r, q); };
  v4u o;
#pragma unroll
  for (int j = 0; j < 4; ++j) o[j] = pk_bf16(dv(rbf(v[2 * j] - mb)), dv(rbf(v[2 * j + 1] - mb)));
  return o;
}
#define WG_BARRIER() do { asm volatile("s_waitcnt lgkmcnt(0)" ::: "memory"); __builtin_amdgcn_s_barrier(); asm volatile("" ::: "memory"); } while (0)
#define VM_DRAIN() do { __builtin_amdgcn_s_waitcnt(0x0f70); asm volatile("" ::: "memory"); } while (0)
#define RSRC(p, n) __builtin_amdgcn_make_buffer_rsrc((void*)(p), 0, (n), 0x00020000)
#define NEXT_SLOT(slot, t) ((t) == 2 ? 3u * 32768u : (slot) >= 2u * 32768u ? 0u : (slot) + 32768u)
#define LDSW(off) (*(volatile LAS unsigned*)(L + (off)))
#define WAIT_GE(off, v, nap) do { while (LDSW(off) < (unsigned)(v)) { if (nap) __builtin_amdgcn_s_sleep(nap); } asm volatile("" ::: "memory"); __builtin_amdgcn_sched_barrier(0); } while (0)
#define LDS_INC(off) do { if (lane == 0) asm volatile("s_waitcnt lgkmcnt(0)\n\tds_add_u32 %0, %1" :: "v"((unsigned)(__UINTPTR_TYPE__)(L + (off))), "v"(1u) : "memory"); } while (0)
#define SET_FLAG(v) do { asm volatile("s_waitcnt lgkmcnt(0)" ::: "memory"); if (lane == 0) LDSW(L_FLAG) = (v); } while (0)

__global__ void __launch_bounds__(NTHR, 2) fused_decode(const u16* __restrict__ x, const u16* __restrict__ kc, const u16* __restrict__ vc, const u16* __restrict__ wq,
                                                        const u16* __restrict__ wo, const u16* __restrict__ wgt, const u16* __restrict__ wup, const u16* __restrict__ wdn,
                                                        u16* __restrict__ out, char* __restrict__ ws) {
  extern __shared__ __attribute__((aligned(16))) char smem[];
  LAS char* L = (LAS char*)smem;
  const unsigned tstart = (unsigned)__builtin_amdgcn_s_memrealtime();
  asm volatile("" :: "s"(kc), "s"(vc), "s"(wq), "s"(x), "s"(ws), "s"(tstart));
  const int lane = threadIdx.x & 63, wave = __builtin_amdgcn_readfirstlane(threadIdx.x >> 6);
  const int wg = blockIdx.x, idx = wg >> 3;
  const int h = (wg & 1) | ((idx >> 1) << 1), sp = idx & 1, b = (wg >> 1) & 3;
  const int prod = h * 2 + sp;
  const int ks = 2 * b + sp;
  const int g = lane >> 4, c = lane & 15;
  if (wave >= NLOAD) {
  if (wave != HELPER_WAVE) {
    const int w = wave - CONS0;
    v4u xr = *(const GAS v4u*)(x + w * DM + lane * 8);
    __builtin_amdgcn_sched_barrier(0);
    __builtin_amdgcn_s_barrier();
    asm volatile("" : "+v"(xr) :: "memory");
    *(LAS v4u*)(L + L_XN + w * 1024 + lane * 16) = layernorm(xr);
    LDS_INC(L_RDY);
    WAIT_GE(L_FLAG, 1, 1);
    const v4u q = *(LAS v4u*)(L + L_Q + c * 16);
    const unsigned tg = LDSW(L_TAG);
    float m = -INFINITY, l = 0.f;
    v2f acc[4] = {{0.f, 0.f}, {0.f, 0.f}, {0.f, 0.f}, {0.f, 0.f}};
    const bool c8 = c & 8, c4 = c & 4;
    unsigned slot = 0;
    LAS char* const lk = L + L_RING + w * 4096 + lane * 16;
#define PV2(k) do { const v2f p_ = {pt[k], pt[k]};                                                                  \
      _Pragma("unroll") for (int j = 0; j < 4; ++j) acc[j] = __builtin_elementwise_fma(p_, v2f{bf_lo(vb[k][j]), bf_hi(vb[k][j])}, acc[j]); } while (0)
#define ROUNDS(ta, tb, nap) _Pragma("unroll 1") for (int t = (ta); t < (tb); ++t) {                                  \
      v4u kb[4], vb[4];                                                                                              \
      WAIT_GE(L_LAND, t * 16 + 4 * w + 4, nap);                                                                      \
      _Pragma("unroll") for (int j = 0; j < 4; ++j) kb[j] = *(LAS v4u*)(lk + slot + j * 1024);                      \
      float d_[4];                                                                                                   \
      _Pragma("unroll") for (int j = 0; j < 4; ++j) d_[j] = dot2(kb[j].w, q.w, dot2(kb[j].z, q.z, dot2(kb[j].y, q.y, dot2(kb[j].x, q.x, 0.f)))); \
      const float e0 = (c8 ? d_[2] : d_[0]) + dpp<ROR8>(c8 ? d_[0] : d_[2]);                                        \
      const float e1 = (c8 ? d_[3] : d_[1]) + dpp<ROR8>(c8 ? d_[1] : d_[3]);                                        \
      float sc = (c4 ? e1 : e0) + dpp<XOR7>(c4 ? e0 : e1);                                                          \
      sc += dpp<XOR2>(sc); sc += dpp<XOR1>(sc);                                                                     \
      sc = rbf(rbf(sc) * SCALE);                                                                                     \
      float tm = fmaxf(sc, dpp<ROR4>(sc)); tm = fmaxf(tm, dpp<ROR8>(tm));                                           \
      if (__builtin_amdgcn_ballot_w64(tm > m)) {                                                                     \
        const float mn = fmaxf(m, tm), al = __builtin_amdgcn_exp2f((m - mn) * LOG2E);                               \
        m = mn; l *= al;                                                                                             \
        _Pragma("unroll") for (int j = 0; j < 4; ++j) acc[j] *= al;                                                 \
      }                                                                                                              \
      const float pe = __builtin_amdgcn_exp2f((sc - m) * LOG2E);                                                    \
      l += pe;                                                                                                       \
      float pt[4] = {dpp<BCAST + 0>(pe), dpp<BCAST + 4>(pe), dpp<BCAST + 8>(pe), dpp<BCAST + 12>(pe)};              \
      asm volatile("" : "+v"(pt[0]), "+v"(pt[1]), "+v"(pt[2]), "+v"(pt[3]));                                        \
      WAIT_GE(L_LAND + 4, t * 16 + 4 * w + 2, nap);                                                                  \
      vb[0] = *(LAS v4u*)(lk + slot + 16384); vb[1] = *(LAS v4u*)(lk + slot + 17408);                               \
      PV2(0); PV2(1);                                                                                                \
      asm volatile("" : "+v"(acc[0]), "+v"(acc[1]), "+v"(acc[2]), "+v"(acc[3]));                                    \
      WAIT_GE(L_LAND + 4, t * 16 + 4 * w + 4, nap);                                                                  \
      vb[2] = *(LAS v4u*)(lk + slot + 18432); vb[3] = *(LAS v4u*)(lk + slot + 19456);                               \
      asm volatile("s_waitcnt lgkmcnt(0)" : "+v"(vb[2]), "+v"(vb[3]) :: "memory");                                  \
      if (lane == 0) LDSW(L_DONE + w * 4) = t + 1;                                                                   \
      __builtin_amdgcn_sched_barrier(0);                                                                             \
      PV2(2); PV2(3);                                                                                                \
      slot = NEXT_SLOT(slot, t);                                                                                     \
    }
    ROUNDS(0, NT, 1)
    {
      const float mw = xrow_max(m), f = __builtin_amdgcn_exp2f((m - mw) * LOG2E);
      float lw = l * f; lw += dpp<ROR4>(lw); lw += dpp<ROR8>(lw); lw = xrow_sum(lw);
      float u[4], v[2];
#pragma unroll
      for (int j = 0; j < 4; ++j) { auto sw = __builtin_amdgcn_permlane16_swap(asu(acc[j >> 1][j & 1] * f), asu(acc[2 + (j >> 1)][j & 1] * f), false, false); u[j] = asf(sw[0]) + asf(sw[1]); }
#pragma unroll
      for (int j = 0; j < 2; ++j) { auto sw = __builtin_amdgcn_permlane32_swap(asu(u[j]), asu(u[j + 2]), false, false); v[j] = asf(sw[0]) + asf(sw[1]); }
      *(LAS v2f*)(L + L_O + w * 512 + c * 32 + (g & 1) * 16 + (g & 2) * 4) = v2f{v[0], v[1]};
      if (lane == 0) *(LAS v2f*)(L + L_ML + w * 8) = v2f{mw, lw};
    }
    WG_BARRIER();
    {
      const auto ro = RSRC(ws + WS_O, NH * (8192 + 128));
      const unsigned so_ = h * 8192 + w * 2048, ss_ = NH * 8192 + h * 128 + w * 32;
      unsigned spins = 0;
      v4u a0, a1, as, b0, b1, bs;
#define O_LOAD(x0, x1, xs) do { asm volatile("" ::: "memory"); xs = __builtin_amdgcn_raw_buffer_load_b128(ro, (lane & 1) * 16, ss_, SC1_AUX); \
        x0 = __builtin_amdgcn_raw_buffer_load_b128(ro, lane * 16, so_, SC1_AUX); x1 = __builtin_amdgcn_raw_buffer_load_b128(ro, lane * 16 + 1024, so_, SC1_AUX); __builtin_amdgcn_sched_barrier(0); } while (0)
#define O_BAD(x0, x1, xs) (__builtin_amdgcn_ballot_w64(((xs.y ^ tg) | (xs.w ^ tg) | (x0.y ^ tg) | (x0.w ^ tg) | (x1.y ^ tg) | (x1.w ^ tg)) != 0) != 0)
      __builtin_amdgcn_s_sleep(10);
      O_LOAD(a0, a1, as);
      __builtin_amdgcn_s_sleep(6);
      for (;;) {
        O_LOAD(b0, b1, bs);
        if (!O_BAD(a0, a1, as) || ++spins >= SPIN_CAP) break;
        if (spins < 2) __builtin_amdgcn_s_sleep(8);
        O_LOAD(a0, a1, as);
        if (!O_BAD(b0, b1, bs)) { a0 = b0; a1 = b1; as = bs; break; }
        if (spins < 2) __builtin_amdgcn_s_sleep(8);
      }
      const float wl = __builtin_amdgcn_rcpf(1.f + __builtin_amdgcn_exp2f(dpp<XOR1>(asf(as.x)) - asf(as.x))), wo_ = dpp<XOR1>(wl);
      const float wa = (lane & 1) ? wo_ : wl, wb = (lane & 1) ? wl : wo_;
      *(LAS unsigned*)(L + L_A + w * AROW + lane * 4) = pk_bf16(fmaf(wa, asf(a0.x), wb * asf(a1.x)), fmaf(wa, asf(a0.z), wb * asf(a1.z)));
      LDS_INC(L_OROW);
    }
  } else {
    __builtin_amdgcn_s_setprio(2);
    unsigned tg = *(const GAS unsigned*)(ws + WS_EPOCH);
    unsigned xw = *(const GAS unsigned*)(x + b * DM + prod * 8 + (lane & 3) * 2);
    __builtin_amdgcn_sched_barrier(0);
    __builtin_amdgcn_s_barrier();
    VM_DRAIN();
    asm volatile("" : "+v"(xw), "+v"(tg));
    tg += 1u;
    if (lane == 0) LDSW(L_TAG) = tg;
    const v4f z4 = {0.f, 0.f, 0.f, 0.f};
    LAS char* const a_xn = L + L_XN + (lane & 3) * 1024 + g * 16;
    const int trow = 8 * g + (c >> 2);
    unsigned spins = 0;
#define POLL_FAILED(bad) (__builtin_amdgcn_ballot_w64(bad) != 0 && ++spins < SPIN_CAP)
    {
      WAIT_GE(L_RDY, 6, 1);
      LAS char* b0 = L + L_WQ + trow * 64 + 16 * (c & 3);
      v4u fa[8], fb[8][2];
#pragma unroll
      for (int i = 0; i < 8; ++i) { fa[i] = *(LAS v4u*)(a_xn + (b >> 1) * 512 + i * 64); fb[i][0] = tr_frag(b0 + i * 2048, b0 + i * 2048 + 256); fb[i][1] = tr_frag(b0 + i * 2048 + 8, b0 + i * 2048 + 264); }
      __builtin_amdgcn_sched_barrier(0);
      v4f d0 = z4, d1 = z4;
#pragma unroll
      for (int i = 0; i < 8; ++i) { d0 = mfma(fa[i], fb[i][0], d0); d1 = mfma(fa[i], fb[i][1], d1); }
      const auto rq = RSRC(ws + WS_Q + h * NB * 2048, NB * 2048);
      __builtin_amdgcn_raw_buffer_store_b128(make_unit(pick4(d0, g), pick4(d1, g), c, tg, tg), rq, g * 2048 + (b >> 1) * 1024 + ((b & 1) * 2 + sp) * 256 + (c >> 2) * 64 + unit_idx(c) * 16, 0, SC1_AUX);
      __builtin_amdgcn_s_sleep(20);
      v4u u0, u1;
      do {
        asm volatile("" ::: "memory");
        u0 = __builtin_amdgcn_raw_buffer_load_b128(rq, b * 2048 + lane * 16, 0, SC1_AUX); u1 = __builtin_amdgcn_raw_buffer_load_b128(rq, b * 2048 + 1024 + lane * 16, 0, SC1_AUX);
      } while (POLL_FAILED(((u0.y ^ tg) | (u0.w ^ tg) | (u1.y ^ tg) | (u1.w ^ tg)) != 0));
      *(LAS unsigned*)(L + L_Q + lane * 4) = pk_bf16(asf(u0.x) + asf(u1.x), asf(u0.z) + asf(u1.z));
      SET_FLAG(1u);
    }
    while (min(min(LDSW(L_DONE), LDSW(L_DONE + 4)), min(LDSW(L_DONE + 8), LDSW(L_DONE + 12))) < 4u) __builtin_amdgcn_s_sleep(4);
    asm volatile("" ::: "memory");
#define SIDE_LOAD(SG, rs, n, ldsstep, voff, goff, gstep, aux) do { _Pragma("unroll 1") for (int i0 = 0; i0 < (n); i0 += (SG)) { \
        _Pragma("unroll") for (int i = 0; i < (SG); ++i) __builtin_amdgcn_raw_ptr_buffer_load_lds(rs, (LAS void*)(L + L_W + (i0 + i) * (ldsstep)), 16, voff, (goff) + (i0 + i) * (gstep), 0, aux); \
        asm volatile("s_waitcnt vmcnt(%0)" :: "i"(SG) : "memory"); } VM_DRAIN(); } while (0)
    if (idx >= 1 && idx < 8) {
      const int fi = (wg & 7) * 7 + idx - 1, mat = fi & 1, col0 = (fi >> 1) * 64;
      const unsigned vf = (lane >> 3) * (FF * 2) + (lane & 7) * 16;
      const auto rw = RSRC((mat ? wup : wgt) + col0, (DM * FF - col0) * 2);
      v4f dg[4] = {z4, z4, z4, z4};
      LAS char* b0 = L + L_W + trow * 128 + 8 * (c & 3);
#pragma unroll 1
      for (int half = 0; half < 2; ++half) {
        SIDE_LOAD(SIDE_G, rw, 32, 1024, vf, half * 256 * FF * 2, 8 * FF * 2, NT_AUX);
#pragma unroll 2
        for (int i = 0; i < 8; ++i) {
          const v4u a = *(LAS v4u*)(a_xn + (half * 8 + i) * 64);
#pragma unroll
          for (int n = 0; n < 4; ++n) dg[n] = mfma(a, tr_frag(b0 + i * 4096 + 32 * n, b0 + i * 4096 + 512 + 32 * n), dg[n]);
        }
        asm volatile("s_waitcnt lgkmcnt(0)" : "+v"(dg[0]), "+v"(dg[1]), "+v"(dg[2]), "+v"(dg[3]) :: "memory");
      }
      const auto rh = RSRC(ws + WS_H, 2 * NB * FF * 4);
#pragma unroll
      for (int n = 0; n < 4; ++n) {
        const v4u unit = make_unit_bf16(pick4(dg[n], g), tg);
        if ((c & 3) == 0) __builtin_amdgcn_raw_buffer_store_b128(unit, rh, (mat * NB + g) * (FF * 4) + (col0 + 16 * n + c) * 4, 0, SC1_AUX);
      }
    } else if (idx >= 8 && idx < 15) {
      const int col0 = (wg & 7) * 64, rg = idx - 8;
      const auto rd = RSRC(wdn + (size_t)rg * 256 * DM + col0, (256 * DM - col0) * 2);
      SIDE_LOAD(SIDE_G, rd, 32, 1024, (lane >> 3) * (DM * 2) + (lane & 7) * 16, 0, 8 * DM * 2, NT_AUX);
      WAIT_GE(L_LAND, H_POLL_R * 16, 8);
      const auto rh = RSRC(ws + WS_H, 2 * NB * FF * 4);
      v4u u[8]; unsigned bad;
      do {
        asm volatile("" ::: "memory");
#pragma unroll
        for (int i = 0; i < 8; ++i) u[i] = __builtin_amdgcn_raw_buffer_load_b128(rh, rg * 1024 + lane * 16, i * (FF * 4), SC1_AUX);
        bad = 0;
#pragma unroll
        for (int i = 0; i < 8; ++i) bad |= (u[i].y ^ tg) | (u[i].w ^ tg);
        if (__builtin_amdgcn_ballot_w64(bad != 0)) __builtin_amdgcn_s_sleep(100);
      } while (POLL_FAILED(bad != 0));
#pragma unroll
      for (int r = 0; r < NB; ++r) {
        const v4u gq = u[r], uq = u[NB + r];
        *(LAS v2u*)(L + L_H + r * HROW + lane * 8) = v2u{pk_bf16(rbf(silu(bf_lo(gq.x))) * bf_lo(uq.x), rbf(silu(bf_hi(gq.x))) * bf_hi(uq.x)),
                                                         pk_bf16(rbf(silu(bf_lo(gq.z))) * bf_lo(uq.z), rbf(silu(bf_hi(gq.z))) * bf_hi(uq.z))};
      }
      LAS char* a0 = L + L_H + (lane & 3) * HROW + g * 16;
      LAS char* b0 = L + L_W + trow * 128 + 16 * (c & 3);
      v4f d[2][2] = {{z4, z4}, {z4, z4}};
#pragma unroll 2
      for (int i = 0; i < 8; ++i) {
        const v4u a = *(LAS v4u*)(a0 + i * 64);
#pragma unroll
        for (int qq = 0; qq < 2; ++qq)
#pragma unroll
          for (int e = 0; e < 2; ++e) d[qq][e] = mfma(a, tr_frag(b0 + i * 4096 + 64 * qq + 8 * e, b0 + i * 4096 + 512 + 64 * qq + 8 * e), d[qq][e]);
      }
      const auto rf = RSRC(ws + WS_G, NB * 64 * 512);
#pragma unroll
      for (int qq = 0; qq < 2; ++qq)
        __builtin_amdgcn_raw_buffer_store_b128(make_unit(pick4(d[qq][0], g), pick4(d[qq][1], g), c, tg, tg), rf, (g * 64 + (col0 >> 3) + 4 * qq + (c >> 2)) * 512 + rg * 64 + unit_idx(c) * 16, 0, SC1_AUX);
      asm volatile("s_waitcnt lgkmcnt(0)" ::: "memory");
    }
    float gate0, gate1;
    {
      const auto rf = RSRC(ws + WS_G + (b * 64 + prod) * 512, 512);
      WAIT_GE(L_LAND, G_POLL_R * 16, 8);
      const bool on = lane < 28;
      v4u u;
      do { asm volatile("" ::: "memory"); u = __builtin_amdgcn_raw_buffer_load_b128(rf, (lane & 31) * 16, 0, SC1_AUX); } while (POLL_FAILED(on && (u.y != tg || u.w != tg)));
      float f0 = on ? asf(u.x) : 0.f, f1 = on ? asf(u.z) : 0.f;
      f0 += dpp<ROR4>(f0); f0 += dpp<ROR8>(f0); f0 = xrow_sum(f0);
      f1 += dpp<ROR4>(f1); f1 += dpp<ROR8>(f1); f1 = xrow_sum(f1);
      gate0 = rbf(silu(rbf(f0))); gate1 = rbf(silu(rbf(f1)));
      asm volatile("" : "+v"(gate0), "+v"(gate1));
    }
    {
      const auto ro = RSRC(wo + (size_t)h * DH * DM + ks * 64, (DH * DM - ks * 64) * 2);
      WAIT_GE(L_LAND, NT * 16 - 4, 2);
#pragma unroll 1
      for (int i = 0; i < 16; ++i) __builtin_amdgcn_raw_ptr_buffer_load_lds(ro, (LAS void*)(L + L_W + i * 1024), 16, (lane >> 3) * (DM * 2) + (lane & 7) * 16, i * 8 * DM * 2, 0, NT_AUX);
    }
    WG_BARRIER();
    {
      const auto ro = RSRC(ws + WS_O, NH * (8192 + 128));
      {
        const v4f ml0 = *(LAS v4f*)(L + L_ML), ml1 = *(LAS v4f*)(L + L_ML + 16);
        v2f p[4];
#pragma unroll
        for (int i = 0; i < 4; ++i) p[i] = *(LAS v2f*)(L + L_O + i * 512 + lane * 8);
        const float M = fmaxf(fmaxf(ml0[0], ml0[2]), fmaxf(ml1[0], ml1[2]));
        const float c0 = __builtin_amdgcn_exp2f((ml0[0] - M) * LOG2E), c1 = __builtin_amdgcn_exp2f((ml0[2] - M) * LOG2E);
        const float c2 = __builtin_amdgcn_exp2f((ml1[0] - M) * LOG2E), c3 = __builtin_amdgcn_exp2f((ml1[2] - M) * LOG2E);
        const float Ls = fmaf(c0, ml0[1], c1 * ml0[3]) + fmaf(c2, ml1[1], c3 * ml1[3]);
        float inv = __builtin_amdgcn_rcpf(Ls); inv = fmaf(fmaf(-Ls, inv, 1.f), inv, inv);
        const float o0 = (fmaf(c0, p[0][0], c1 * p[1][0]) + fmaf(c2, p[2][0], c3 * p[3][0])) * inv;
        const float o1 = (fmaf(c0, p[0][1], c1 * p[1][1]) + fmaf(c2, p[2][1], c3 * p[3][1])) * inv;
        __builtin_amdgcn_raw_buffer_store_b128(v4u{asu(o0), tg, asu(o1), tg}, ro, ks * 1024 + lane * 16, h * 8192, SC1_AUX);
        const unsigned lu = asu(fmaf(M, LOG2E, __builtin_amdgcn_logf(Ls)));
        if (lane == 0) __builtin_amdgcn_raw_buffer_store_b128(v4u{lu, tg, lu, tg}, ro, ks * 16, NH * 8192 + h * 128, SC1_AUX);
      }
      v4u wf[4][2][2];
      {
        asm volatile("s_waitcnt vmcnt(2)" ::: "memory");
        LAS char* b0 = L + L_W + trow * 128 + 16 * (c & 3);
        const unsigned ab = (unsigned)(__UINTPTR_TYPE__)b0;
#define TRQ(i, qq, e) asm volatile("ds_read_b64_tr_b16 %0, %2 offset:%3\n\tds_read_b64_tr_b16 %1, %2 offset:%4" : "=&v"(lo_[(i) * 4 + (qq) * 2 + (e)]), "=&v"(hi_[(i) * 4 + (qq) * 2 + (e)]) \
          : "v"(ab), "i"((i) * 4096 + 64 * (qq) + 8 * (e)), "i"((i) * 4096 + 512 + 64 * (qq) + 8 * (e)) : "memory")
        v2u lo_[16], hi_[16];
#define TRQ_WAIT(o) asm volatile("s_waitcnt lgkmcnt(0)" : "+v"(lo_[o]), "+v"(lo_[o + 1]), "+v"(lo_[o + 2]), "+v"(lo_[o + 3]), "+v"(lo_[o + 4]), "+v"(lo_[o + 5]), "+v"(lo_[o + 6]), "+v"(lo_[o + 7]), \
          "+v"(hi_[o]), "+v"(hi_[o + 1]), "+v"(hi_[o + 2]), "+v"(hi_[o + 3]), "+v"(hi_[o + 4]), "+v"(hi_[o + 5]), "+v"(hi_[o + 6]), "+v"(hi_[o + 7]) :: "memory")
        TRQ(0, 0, 0); TRQ(0, 0, 1); TRQ(0, 1, 0); TRQ(0, 1, 1); TRQ(1, 0, 0); TRQ(1, 0, 1); TRQ(1, 1, 0); TRQ(1, 1, 1); TRQ_WAIT(0);
        TRQ(2, 0, 0); TRQ(2, 0, 1); TRQ(2, 1, 0); TRQ(2, 1, 1); TRQ(3, 0, 0); TRQ(3, 0, 1); TRQ(3, 1, 0); TRQ(3, 1, 1); TRQ_WAIT(8);
#pragma unroll
        for (int i = 0; i < 16; ++i) wf[i >> 2][(i >> 1) & 1][i & 1] = v4u{lo_[i].x, lo_[i].y, hi_[i].x, hi_[i].y};
      }
      WAIT_GE(L_OROW, 4, 0);
      v4f d[2][2] = {{z4, z4}, {z4, z4}};
#pragma unroll
      for (int i = 0; i < 4; ++i) {
        const v4u a = *(LAS v4u*)(L + L_A + (lane & 3) * AROW + i * 64 + g * 16);
#pragma unroll
        for (int qq = 0; qq < 2; ++qq)
#pragma unroll
          for (int e = 0; e < 2; ++e) d[qq][e] = mfma(a, wf[i][qq][e], d[qq][e]);
      }
      const auto ry = RSRC(ws + WS_Y, NB * 64 * 2048);
#pragma unroll
      for (int qq = 0; qq < 2; ++qq)
        __builtin_amdgcn_raw_buffer_store_b128(make_unit(pick4(d[qq][0], g), pick4(d[qq][1], g), c, tg, tg), ry, (g * 64 + ks * 8 + 4 * qq + (c >> 2)) * 2048 + h * 64 + unit_idx(c) * 16, 0, SC1_AUX);
      v4u y0, y1, z0, z1;
#define Y_LOAD(x0, x1) do { asm volatile("" ::: "memory"); x0 = __builtin_amdgcn_raw_buffer_load_b128(ry, lane * 16, (b * 64 + prod) * 2048, SC1_AUX); \
        x1 = __builtin_amdgcn_raw_buffer_load_b128(ry, lane * 16 + 1024, (b * 64 + prod) * 2048, SC1_AUX); __builtin_amdgcn_sched_barrier(0); } while (0)
#define Y_BAD(x0, x1) (__builtin_amdgcn_ballot_w64(((x0.y ^ tg) | (x0.w ^ tg) | (x1.y ^ tg) | (x1.w ^ tg)) != 0) != 0)
      __builtin_amdgcn_s_sleep(10);
      Y_LOAD(y0, y1);
      __builtin_amdgcn_s_sleep(6);
      for (;;) {
        Y_LOAD(z0, z1);
        if (!Y_BAD(y0, y1) || ++spins >= SPIN_CAP) break;
        Y_LOAD(y0, y1);
        if (!Y_BAD(z0, z1)) { y0 = z0; y1 = z1; break; }
      }
      float s0 = asf(y0.x) + asf(y1.x), s1 = asf(y0.z) + asf(y1.z);
      s0 += dpp<ROR4>(s0); s0 += dpp<ROR8>(s0); s0 = xrow_sum(s0);
      s1 += dpp<ROR4>(s1); s1 += dpp<ROR8>(s1); s1 = xrow_sum(s1);
      const unsigned o = pk_bf16(bf_lo(xw) + rbf(rbf(s0) * gate0), bf_hi(xw) + rbf(rbf(s1) * gate1));
      if (lane < 4) *(GAS unsigned*)(out + b * DM + prod * 8 + lane * 2) = o;
      if (wg == 0 && lane == 0) __hip_atomic_store((GAS unsigned*)(ws + WS_EPOCH), tg, __ATOMIC_RELAXED, __HIP_MEMORY_SCOPE_AGENT);
    }
  }
  } else {
    __builtin_amdgcn_s_setprio(3);
    const unsigned vo = lane * 16;
    const u16* const sbase = (wave ? vc : kc) + (size_t)(b * NH + h) * SK * DH;
    const auto rs = RSRC(sbase, SK * DH * 2);
    unsigned so = sp * 16384, lo = L_RING + wave * 16384;
#define LD_PAIR() do { __builtin_amdgcn_raw_ptr_buffer_load_lds(rs, (LAS void*)(L + lo), 16, vo, so, 0, NT_AUX); \
      __builtin_amdgcn_raw_ptr_buffer_load_lds(rs, (LAS void*)(L + lo), 16, vo, so, 1024, NT_AUX); so += 2048; lo += 2048; } while (0)
    LD_PAIR();
    {
      const int off = (b >> 1) * 256 * NH * DH + h * DH + (b & 1) * 64 + sp * 32;
      const auto rq = RSRC(wq + off, (DM * NH * DH - off) * 2);
      const unsigned v0 = (lane >> 2) * 8192 + (lane & 3) * 16;
#pragma unroll 1
      for (int i = wave * 8; i < wave * 8 + 8; ++i) __builtin_amdgcn_raw_ptr_buffer_load_lds(rq, (LAS void*)(L + L_WQ + i * 1024), 16, v0, i * 16 * 8192, 0, NT_AUX);
    }
    constexpr int PRE = INFL / 2;
#pragma unroll 1
    for (int n = 1; n < PRE; ++n) LD_PAIR();
    if (wave == 0 && lane < 9) LDSW(L_DONE + lane * 4) = 0u;
    WG_BARRIER();
    asm volatile("s_waitcnt vmcnt(%0)" :: "i"(2 * PRE - 2) : "memory");
    LDS_INC(L_RDY);
    const unsigned a_done = (unsigned)(__UINTPTR_TYPE__)(L + L_DONE);
    volatile LAS unsigned* const land = (volatile LAS unsigned*)(L + L_LAND + wave * 4);
    unsigned lfloor = 0;
#define LANDED(n) do { lfloor = max((unsigned)(n), lfloor); if (lane == 0) *land = lfloor; } while (0)
    const v4u rsv = {(unsigned)(__UINTPTR_TYPE__)sbase, (unsigned)((__UINTPTR_TYPE__)sbase >> 32) & 0xffffu, SK * DH * 2, 0x00020000u};
    const unsigned a_land = (unsigned)(__UINTPTR_TYPE__)land;
    int lvl = 0;
    int cnt = 2 * PRE + 1, left = 8 - PRE;
    const unsigned l0 = (unsigned)(__UINTPTR_TYPE__)L + wave * 16384;
    unsigned slot = 0;
    lo += (unsigned)(__UINTPTR_TYPE__)L;
#define ROUND_ASM(WIN) \
      asm volatile( \
          "s_memrealtime s[60:61]\n\t" \
          "s_mov_b32 m0, %[lo0]\n\t" \
          "s_mov_b32 %[lo], %[lo0]\n\t" \
          "s_mov_b32 %[so], %[so0]\n\t" \
          "s_mov_b32 %[cnt], %[cnt0]\n\t" \
          "s_mov_b32 %[left], %[left0]\n" \
          "1:\n\t" \
          "s_waitcnt vmcnt(%[w])\n\t" \
          "buffer_load_dwordx4 %[vo], %[rs], %[so] offen nt lds\n\t" \
          "s_max_i32 %[pub], %[cnt], %[fl]\n\t" \
          "v_mov_b32 %[vt], %[pub]\n\t" \
          "s_mov_b64 exec, 1\n\t" \
          "ds_write_b32 %[al], %[vt]\n\t" \
          "s_mov_b64 exec, -1\n\t" \
          "ds_read_b128 %[dn], %[ad]\n\t" \
          "s_add_i32 %[cnt], %[cnt], 2\n\t" \
          "s_add_i32 %[lo], %[lo], 0x800\n\t" \
          "s_waitcnt vmcnt(%[w])\n\t" \
          "buffer_load_dwordx4 %[vo], %[rs], %[so] offen offset:1024 nt lds\n\t" \
          "s_add_i32 %[so], %[so], 0x800\n\t" \
          "s_mov_b32 m0, %[lo]\n\t" \
          "s_sub_i32 %[left], %[left], 1\n\t" \
          "s_cmp_lg_u32 %[left], 0\n\t" \
          "s_cbranch_scc1 1b\n\t" \
          "s_waitcnt lgkmcnt(0)\n\t" \
          "v_mov_b32 %[vt], s60" \
          : [lo] "=&s"(t0_), [so] "=&s"(t1_), [cnt] "=&s"(t2_), [left] "=&s"(t3_), [pub] "=&s"(pub), [vt] "=&v"(vt), [dn] "=&v"(dn) \
          : [lo0] "s"(lo), [so0] "s"(so), [cnt0] "s"(cnt - (WIN)), [left0] "s"(left), [vo] "v"(vo), [rs] "s"(rsv), [fl] "s"(lfloor), [al] "v"(a_land), [ad] "v"(a_done), [w] "i"((WIN) - 1) \
          : "memory", "scc", "s60", "s61")
#pragma unroll 1
    for (int t = 0; t < NT; ++t) {
      v4u dn; unsigned vt, pub, t0_, t1_, t2_, t3_;
      if (lvl == 0) { ROUND_ASM(INFL); } else if (lvl == 1) { ROUND_ASM(W1); } else if (lvl == 2) { ROUND_ASM(W2); } else { ROUND_ASM(W3); }
      {
        const int lag = (int)(__builtin_amdgcn_readfirstlane(vt) - tstart) - (T_S + ((t * TAU_Q8S) >> 8));
        lvl = (lag > LAG1) + (lag > LAG2) + (lag > LAG3);
      }
      lfloor = max(lfloor, (unsigned)max(16 * t + 15 - W3, 0));
      left = 8; cnt = 16 * t + 17; so = sp * 16384 + (t + 1) * 32768;
      slot = NEXT_SLOT(slot, t); lo = l0 + slot;
      {
        const unsigned need = t == NT - 1 ? 0 : max(t - (t < 6 ? 2 : 1), 0);
        while (__builtin_amdgcn_readfirstlane(min(min(dn.x, dn.y), min(dn.z, dn.w))) < need) {
          asm volatile("s_waitcnt vmcnt(0)" ::: "memory");
          LANDED(t * 16 + 16);
          asm volatile("ds_read_b128 %0, %1\n\ts_waitcnt lgkmcnt(0)" : "=v"(dn) : "v"(a_done) : "memory");
        }
      }
    }
#pragma unroll
    for (int r = (W3 - 1) / 2; r >= 0; --r) { asm volatile("s_waitcnt vmcnt(%0)" :: "i"(2 * r) : "memory"); LANDED(NT * 16 - 2 * r); }
    WG_BARRIER();
  }
}

extern "C" void kernel_launch(void* const* d_in, const int* in_sizes, int n_in, void* d_out, int out_size, void* d_ws, size_t ws_size, hipStream_t stream) {
  (void)in_sizes; (void)n_in; (void)out_size;
  if (ws_size < WS_END) return;
  (void)hipFuncSetAttribute((const void*)fused_decode, hipFuncAttributeMaxDynamicSharedMemorySize, L_END);
  fused_decode<<<NWG, NTHR, L_END, stream>>>((const u16*)d_in[0], (const u16*)d_in[1], (const u16*)d_in[2], (const u16*)d_in[3], (const u16*)d_in[4],
                                             (const u16*)d_in[6], (const u16*)d_in[7], (const u16*)d_in[8], (u16*)d_out, (char*)d_ws);
}
```
